```python
import torch

def _softmax(x, axis=-1):
    return torch.softmax(x, dim=axis)
def _gelu(x):
    return torch.nn.functional.gelu(x, approximate="tanh")
def _silu(x):
    return torch.nn.functional.silu(x)
def _sigmoid(x):
    return torch.sigmoid(x)
def _layernorm(x, axis=-1, eps=1e-5):
    m = x.mean(dim=axis, keepdim=True)
    v = x.var(dim=axis, keepdim=True, unbiased=False)
    return (x - m) / torch.sqrt(v + eps)
def _scatter_add(tgt, idx, src, axis=0):
    return tgt.clone().index_add_(axis, idx, src)
def _segment_sum(src, seg, n):
    out = torch.zeros((n,) + src.shape[1:], dtype=src.dtype)
    return out.index_add_(0, seg, src)
def _pad(x, pw):
    flat = tuple(v for pair in reversed(pw) for v in pair)
    return torch.nn.functional.pad(x, flat)

kernel_name = 'parallel_sum__compressed_kv_cla__dense_activation_gelu__prompt'

def setup_inputs():

    _w = (lambda a: (lambda t: t.to(torch.bfloat16) if t.is_floating_point() else t)(torch.as_tensor(a)))
    def _A_setup():
        B, H, Sq, Sk, D, M = 1, 32, 2048, 2048, 128, 512
        Dc = 4 * D
        return {
            "x": _w((lambda *s: torch.randn(*s, dtype=torch.bfloat16) * 0.05)(B, Sq, M)),
            "c_kv": _w((lambda *s: torch.randn(*s, dtype=torch.bfloat16) * 0.4)(B, Sk, Dc)),
            "w_q": _w((lambda *s: torch.randn(*s, dtype=torch.bfloat16) * 3.2)(M, H, D) * (M ** -0.5)),
            "w_uk": _w((lambda *s: torch.randn(*s, dtype=torch.bfloat16) * 3.2)(Dc, H, D) * (Dc ** -0.5)),
            "w_uv": _w((lambda *s: torch.randn(*s, dtype=torch.bfloat16) * 0.4)(Dc, H, D) * (Dc ** -0.5)),
            "w_o": _w((lambda *s: torch.randn(*s, dtype=torch.bfloat16) * 0.4)(H, D, M) * ((H * D) ** -0.5)),
        }
    _A_in = _A_setup()

    def _B_setup():
        B, S, D, F = 1, 2048, 512, 1376
        return {
            "x": _w((lambda *s: torch.randn(*s, dtype=torch.bfloat16) * 0.05)(B, S, D)),
            "w1": _w((lambda *s: torch.randn(*s, dtype=torch.bfloat16) * 0.4)(D, F) * (D ** -0.5)),
            "b1": _w((lambda *s: torch.randn(*s, dtype=torch.bfloat16) * 12.8)(F) * 0.01),
            "w2": _w((lambda *s: torch.randn(*s, dtype=torch.bfloat16) * 0.2)(F, D) * (F ** -0.5)),
            "b2": _w((lambda *s: torch.randn(*s, dtype=torch.bfloat16) * 3.2)(D) * 0.01),
        }
    _B_in = _B_setup()

    return {
        "A_x": _A_in["x"],
        "A_c_kv": _A_in["c_kv"],
        "A_w_q": _A_in["w_q"],
        "A_w_uk": _A_in["w_uk"],
        "A_w_uv": _A_in["w_uv"],
        "A_w_o": _A_in["w_o"],
        "B_x": _B_in["x"],
        "B_w1": _B_in["w1"],
        "B_b1": _B_in["b1"],
        "B_w2": _B_in["w2"],
        "B_b2": _B_in["b2"],
    }

def reference(A_x, A_c_kv, A_w_q, A_w_uk, A_w_uv, A_w_o, B_x, B_w1, B_b1, B_w2, B_b2):
    def _A_ref(x, c_kv, w_q, w_uk, w_uv, w_o):
        B, Sq, M = x.shape[0], x.shape[1], x.shape[2]
        Sk = c_kv.shape[1]
        H, D = w_q.shape[1], w_q.shape[2]
        q = torch.einsum("bqm,mhd->bhqd", x, w_q)
        k = torch.einsum("bkc,chd->bhkd", c_kv, w_uk)
        v = torch.einsum("bkc,chd->bhkd", c_kv, w_uv)
        scores = torch.einsum("bhqd,bhkd->bhqk", q, k) * (D ** -0.5)
        off = Sk - Sq
        qi = torch.arange(Sq)[:, None]
        kj = torch.arange(Sk)[None, :]
        cmask = kj <= (qi + off)
        scores = torch.where(cmask[None, None], scores, -1e9)
        p = _softmax(scores, axis=-1)
        ctx = torch.einsum("bhqk,bhkd->bhqd", p, v)
        return torch.einsum("bhqd,hdm->bqm", ctx, w_o)

    def _B_ref(x, w1, b1, w2, b2):
        h = torch.einsum("bsd,df->bsf", x, w1) + b1
        h = _gelu(h)
        return torch.einsum("bsf,fd->bsd", h, w2) + b2

    x = A_x
    xn = _layernorm(x, axis=-1)
    return x + _A_ref(xn, A_c_kv, A_w_q, A_w_uk, A_w_uv, A_w_o) + _B_ref(xn, B_w1, B_b1, B_w2, B_b2)
```

```cpp
#include <hip/hip_runtime.h>
#include <hip/hip_bf16.h>
#include <stdint.h>

typedef __hip_bfloat16 bf16;
typedef short bf16x8 __attribute__((ext_vector_type(8)));
typedef short s16x4 __attribute__((ext_vector_type(4)));
typedef float f32x4 __attribute__((ext_vector_type(4)));
typedef float f32x16 __attribute__((ext_vector_type(16)));
typedef unsigned u32x4 __attribute__((ext_vector_type(4)));
typedef unsigned u32x2 __attribute__((ext_vector_type(2)));
typedef __attribute__((address_space(3))) s16x4* lds_tr_ptr;

constexpr int SEQ = 2048, NH = 32, HD = 128, DM = 512, FF = 1376;
constexpr int A3_LD = 5504;

constexpr size_t WS_XN = 0;
constexpr size_t WS_K = WS_XN + (size_t)SEQ * DM * 2;
constexpr size_t WS_V = WS_K + (size_t)NH * SEQ * HD * 2;
constexpr size_t WS_A3 = WS_V + (size_t)NH * SEQ * HD * 2;
constexpr size_t WS_P3 = WS_A3 + (size_t)SEQ * A3_LD * 2;
constexpr size_t WS_END = WS_P3 + (size_t)4 * SEQ * DM * 4;

#define STAMP(buf, idx) do {} while (0)
#define TACC(acc, prev) do {} while (0)
constexpr size_t WS_DBG = WS_END + 4096;
#define TRRD(p) __builtin_amdgcn_ds_read_tr16_b64_v4i16((lds_tr_ptr)(p))
template <int OFF>
__device__ __forceinline__ s16x4 tr_asm(unsigned addr) {
  s16x4 r;
  asm volatile("ds_read_b64_tr_b16 %0, %1 offset:%2" : "=&v"(r) : "v"(addr), "i"(OFF));
  return r;
}
__device__ __forceinline__ unsigned lds_addr(const void* p) {
  return (unsigned)(uintptr_t)(const __attribute__((address_space(3))) char*)p;
}

__device__ __forceinline__ float bf2f(unsigned short u) { return __uint_as_float((unsigned)u << 16); }
__device__ __forceinline__ float bfr(float x) { return (float)(__bf16)x; }
__device__ __forceinline__ unsigned pk2(float a, float b) {
  typedef __bf16 b2 __attribute__((ext_vector_type(2)));
  b2 v = {(__bf16)a, (__bf16)b};
  return __builtin_bit_cast(unsigned, v);
}
__device__ __forceinline__ bf16x8 cat8(s16x4 lo, s16x4 hi) { return __builtin_shufflevector(lo, hi, 0, 1, 2, 3, 4, 5, 6, 7); }
__device__ __forceinline__ int swap23(int d) { return (d & ~12) | ((d & 4) << 1) | ((d & 8) >> 1); }

namespace k1 {
constexpr int BM = 256, BK = 64, HALF = 128, HT = HALF * BK, LDS_BYTES = 8 * HT * 2;
__device__ __forceinline__ int lds_byte(int r, int c) {
  int st = (r >> 4) * 2 + (c >> 5), rr = r & 15, cc = c & 31, ob = rr * 64 + cc * 2;
  return st * 1024 + (ob ^ (((ob >> 9) & 1) << 5));
}
__device__ __forceinline__ void stage_rc(int b, int& R, int& C) {
  int st = b / 1024, sb = b % 1024, swz = sb ^ (((sb >> 9) & 1) << 5);
  R = (st >> 1) * 16 + swz / 64;
  C = (st & 1) * 32 + (swz % 64) / 2;
}
__device__ __forceinline__ void stage_kn(int b, int& Kr, int& Nc) {
  int blk = b >> 8, kb = blk >> 3, nb = blk & 7, slot = (b >> 5) & 7, hc = (b >> 4) & 1;
  Kr = kb * 8 + (slot ^ ((kb & 1) << 2));
  Nc = nb * 16 + hc * 8;
}
}

__global__ __launch_bounds__(512, 2) void k1_kvproj(const bf16* __restrict__ Ckv, const bf16* __restrict__ Wuk,
                                                    const bf16* __restrict__ Wuv, bf16* __restrict__ Kp, bf16* __restrict__ Vp,
                                                    const bf16* __restrict__ X, bf16* __restrict__ Xn, bf16* __restrict__ A3) {
  using namespace k1;
  STAMP(dbg, 0);
  extern __shared__ __attribute__((aligned(16))) bf16 shm[];
  constexpr int K = DM;
#define SA(b, h) (shm + ((b) * 2 + (h)) * HT)
#define SB(b, h) (shm + (4 + (b) * 2 + (h)) * HT)
#define STAGEA_X(T, P, br, kt)                                                                           \
  do {                                                                                                   \
    long _g = (long)(br) * K + (long)(kt) * BK;                                                          \
    for (int _i = 0; _i < 2; ++_i) {                                                                     \
      int _b = (T) * 16 + _i * 8192;                                                                     \
      int _r, _c;                                                                                        \
      stage_rc(_b, _r, _c);                                                                              \
      __builtin_amdgcn_global_load_lds((const unsigned*)(Ckv + _g + (long)_r * K + _c),                  \
                                       (__attribute__((address_space(3))) unsigned*)((char*)(P) + _b), 16, 0, 0); \
    }                                                                                                    \
  } while (0)
#define STAGEB_X(T, P, hf, kt)                                                                           \
  do {                                                                                                   \
    for (int _i = 0; _i < 2; ++_i) {                                                                     \
      int _b = (T) * 16 + _i * 8192;                                                                     \
      int _k, _n;                                                                                        \
      stage_kn(_b, _k, _n);                                                                              \
      __builtin_amdgcn_global_load_lds((const unsigned*)(Wsel + (long)((kt) * BK + _k) * 4096 + bcolW + (hf) * 128 + _n), \
                                       (__attribute__((address_space(3))) unsigned*)((char*)(P) + _b), 16, 0, 0); \
    }                                                                                                    \
  } while (0)
#define STAGEA(P, br, kt) STAGEA_X(threadIdx.x, P, br, kt)
#define STAGEB(P, hf, kt) STAGEB_X(threadIdx.x, P, hf, kt)
#define LDA(dst, b, h)                                                                                   \
  for (int m = 0; m < 4; ++m)                                                                            \
    for (int k = 0; k < 2; ++k)                                                                          \
  dst[m][k] = *reinterpret_cast<const bf16x8*>((char*)SA(b, h) + lds_byte(wr * 64 + m * 16 + fr, k * 32 + fq * 8))
#define LDB(dst, b, h)                                                                                   \
  do {                                                                                                   \
    const unsigned _a0 = sbase + ((4 + (b) * 2 + (h)) * HT * 2) + wb0, _a1 = _a0 ^ 128;                  \
    dst[0][0][0] = tr_asm<0>(_a0); dst[0][0][1] = tr_asm<0>(_a1);                                        \
    dst[0][1][0] = tr_asm<8192>(_a0); dst[0][1][1] = tr_asm<8192>(_a1);                                  \
    dst[1][0][0] = tr_asm<256>(_a0); dst[1][0][1] = tr_asm<256>(_a1);                                    \
    dst[1][1][0] = tr_asm<8448>(_a0); dst[1][1][1] = tr_asm<8448>(_a1);                                  \
  } while (0)
#define MMA(ai, bj, At_, Bt_)                                                                            \
  do {                                                                                                   \
    __builtin_amdgcn_sched_barrier(0);                                                                   \
    __builtin_amdgcn_s_setprio(1);                                                                       \
    for (int m = 0; m < 4; ++m)                                                                          \
      for (int n = 0; n < 2; ++n)                                                                        \
        for (int k = 0; k < 2; ++k)                                                                      \
          acc[ai][bj][m][n] = __builtin_amdgcn_mfma_f32_16x16x32_bf16(cat8(Bt_[n][k][0], Bt_[n][k][1]), At_[m][k], acc[ai][bj][m][n], 0, 0, 0); \
    __builtin_amdgcn_s_setprio(0);                                                                       \
  } while (0)
#define WAIT_V(n) asm volatile("s_waitcnt vmcnt(" #n ")" ::: "memory")
#define WAIT_L(n) asm volatile("s_waitcnt lgkmcnt(" #n ")" ::: "memory")
#define BAR __builtin_amdgcn_s_barrier()
#define SCHED __builtin_amdgcn_sched_barrier(0)

  const int bid = blockIdx.x, xcd = bid & 7, jj = bid >> 3, ctl = jj >> 3, pm = jj & 7;
  const int pn = (ctl < 2) ? (2 * xcd + ctl) : (16 + 2 * xcd + (ctl - 2));
  const bf16* __restrict__ Wsel = (pn < 16) ? Wuk : Wuv;
  const int bcolW = (pn & 15) * 256, brow = pm * BM;
  int wid = threadIdx.x >> 6, lane = threadIdx.x & 63, wr = wid >> 2, wc = wid & 3, fr = lane & 15, fq = lane >> 4;
  const unsigned sbase = lds_addr(shm);
  unsigned wb0 = fq * 2048 + ((lane >> 2) & 3) * 32 + (lane & 3) * 8 + (fq & 1) * 128 + wc * 2 * 256;
  f32x4 acc[2][2][4][2] = {};
  bf16x8 At[4][2];
  s16x4 B0[2][2][2], B1[2][2][2];
  constexpr int nt = K / BK;
  const bf16x8 xv = *reinterpret_cast<const bf16x8*>(X + (size_t)(bid * 8 + wid) * DM + lane * 8);
  STAGEB(SB(0, 0), 0, 0); STAGEA(SA(0, 0), brow, 0);
  STAGEB(SB(0, 1), 1, 0); STAGEA(SA(0, 1), brow + HALF, 0);
  {
    const int row = bid * 8 + wid;
    float f[8], s = 0.f;
    for (int j = 0; j < 8; ++j) { f[j] = bf2f((unsigned short)xv[j]); s += f[j]; }
    for (int o = 32; o; o >>= 1) s += __shfl_xor(s, o);
    const float mean = s * (1.f / DM);
    float vs = 0.f;
    for (int j = 0; j < 8; ++j) { const float d = f[j] - mean; vs += d * d; }
    for (int o = 32; o; o >>= 1) vs += __shfl_xor(vs, o);
    const float mb = bfr(mean), vb = bfr(vs * (1.f / DM));
    const float den = bfr(sqrtf(bfr(vb + 1e-5f)));
    u32x4 o4;
    for (int j = 0; j < 4; ++j) o4[j] = pk2(bfr(f[2 * j] - mb) / den, bfr(f[2 * j + 1] - mb) / den);
    *reinterpret_cast<u32x4*>(Xn + (size_t)row * DM + lane * 8) = o4;
    if (lane < 4) *reinterpret_cast<u32x4*>(A3 + (size_t)row * A3_LD + 4096 + FF + lane * 8) = u32x4{0, 0, 0, 0};
  }
  if (wr == 1) BAR;
  WAIT_V(4); BAR;
  STAGEB(SB(1, 0), 0, 1); STAGEA(SA(1, 0), brow, 1); STAGEB(SB(1, 1), 1, 1);
  WAIT_V(6); BAR;
  STAMP(dbg, 1);
  for (int t = 0; t < nt - 2; t += 2) {
    LDB(B0, 0, 0); SCHED; LDA(At, 0, 0); STAGEA(SA(1, 1), brow + HALF, t + 1);
    WAIT_L(8); BAR; WAIT_L(0); MMA(0, 0, At, B0); BAR; SCHED;
    LDB(B1, 0, 1); STAGEB(SB(0, 0), 0, t + 2);
    BAR; WAIT_L(0); MMA(0, 1, At, B1); BAR;
    LDA(At, 0, 1); STAGEA(SA(0, 0), brow, t + 2);
    BAR; WAIT_L(0); MMA(1, 0, At, B0); BAR; SCHED;
    STAGEB(SB(0, 1), 1, t + 2);
    WAIT_V(6); BAR; MMA(1, 1, At, B1); BAR;
    LDB(B0, 1, 0); SCHED; LDA(At, 1, 0); STAGEA(SA(0, 1), brow + HALF, t + 2);
    WAIT_L(8); BAR; WAIT_L(0); MMA(0, 0, At, B0); BAR; SCHED;
    LDB(B1, 1, 1); STAGEB(SB(1, 0), 0, t + 3);
    BAR; WAIT_L(0); MMA(0, 1, At, B1); BAR;
    LDA(At, 1, 1); STAGEA(SA(1, 0), brow, t + 3);
    BAR; WAIT_L(0); MMA(1, 0, At, B0); BAR; SCHED;
    STAGEB(SB(1, 1), 1, t + 3);
    WAIT_V(6); BAR; MMA(1, 1, At, B1); BAR;
  }
  int tz = threadIdx.x;
  asm volatile("" : "+v"(tz));
  wid = tz >> 6; lane = tz & 63; wr = wid >> 2; wc = wid & 3; fr = lane & 15; fq = lane >> 4;
  wb0 = fq * 2048 + ((lane >> 2) & 3) * 32 + (lane & 3) * 8 + (fq & 1) * 128 + wc * 2 * 256;
  { LDB(B0, 0, 0); WAIT_V(0); LDA(At, 0, 0); STAGEA_X(tz, SA(1, 1), brow + HALF, nt - 1);
    BAR; WAIT_L(0); MMA(0, 0, At, B0); BAR;
    LDB(B1, 0, 1); BAR; WAIT_L(0); MMA(0, 1, At, B1); BAR;
    LDA(At, 0, 1); WAIT_V(4); BAR; WAIT_L(0); MMA(1, 0, At, B0); MMA(1, 1, At, B1); BAR; }
  { LDB(B0, 1, 0); LDA(At, 1, 0); WAIT_V(2); BAR; WAIT_L(0); MMA(0, 0, At, B0); BAR;
    LDB(B1, 1, 1); WAIT_V(0); BAR; WAIT_L(0); MMA(0, 1, At, B1); BAR;
    LDA(At, 1, 1); BAR; WAIT_L(0); MMA(1, 0, At, B0); MMA(1, 1, At, B1); BAR; }
  if (wr == 0) BAR;
  STAMP(dbg, 2);
  {
    const bool isV = pn >= 16;
    bf16* __restrict__ dst = isV ? Vp : Kp;
    const int dlo = isV ? 4 * fq : swap23(4 * fq);
    for (int ai = 0; ai < 2; ++ai)
      for (int bj = 0; bj < 2; ++bj)
        for (int m = 0; m < 4; ++m)
          for (int n = 0; n < 2; ++n) {
            const int rowb = (brow + ai * HALF + wr * 64 + m * 16) >> 4;
            const int col = bcolW + bj * HALF + wc * 32 + n * 16;
            const int head = col >> 7, db = (col & 127) >> 4;
            const f32x4 a = acc[ai][bj][m][n];
            u32x2 v = {pk2(a[0], a[1]), pk2(a[2], a[3])};
            *reinterpret_cast<u32x2*>(dst + (((size_t)head * 128 + rowb) * 8 + db) * 256 + fr * 16 + dlo) = v;
          }
  }
  STAMP(dbg, 3);
  asm volatile("s_waitcnt vmcnt(0)" ::: "memory");
  STAMP(dbg, 4);
#undef SA
#undef SB
#undef STAGEA_X
#undef STAGEB_X
#undef STAGEA
#undef STAGEB
#undef LDA
#undef LDB
#undef MMA
}

namespace k2 {
constexpr int TB = 64 * 128 * 2;
constexpr int OFF_KM = 0, OFF_VM = 2 * TB, OFF_KH = 4 * TB, OFF_VH = 6 * TB, OFF_QS = 8 * TB;
constexpr int OFF_WQ = OFF_KH, OFF_W1 = OFF_KH + 2 * TB;
constexpr int OFF_PART = OFF_KH;
constexpr int LDS_BYTES = 8 * TB + 4 * 8192;
constexpr float CEXP = 0.08838834764831845f * 1.4426950408889634f;
__device__ __forceinline__ int own_f(int h) { return ((43 * h) >> 3) << 3; }
__device__ __forceinline__ float gelu_tanh(float x) {
  const float y = 0.7978845608028654f * (x + 0.044715f * x * x * x);
  const float e = __expf(2.f * y);
  const float t = 1.f - 2.f / (e + 1.f);
  return 0.5f * x * (1.f + t);
}
}

#define EXPF(x) __builtin_amdgcn_exp2f(x)
#define SBAR() __builtin_amdgcn_sched_barrier(0)
#define WAITV0() asm volatile("s_waitcnt vmcnt(0)" ::: "memory")
#define WAITL(n) asm volatile("s_waitcnt lgkmcnt(" #n ")" ::: "memory")
#define RAWBAR() do { asm volatile("s_waitcnt lgkmcnt(0)" ::: "memory"); __builtin_amdgcn_sched_barrier(0); \
                      __builtin_amdgcn_s_barrier(); __builtin_amdgcn_sched_barrier(0); } while (0)
typedef __attribute__((address_space(3))) unsigned* lds_u32_ptr;
__device__ __forceinline__ void dma16(const bf16* g, char* l) {
  __builtin_amdgcn_global_load_lds((const unsigned*)g, (lds_u32_ptr)l, 16, 0, 0);
}
__global__ __launch_bounds__(512, 2) void k2_attn(const bf16* __restrict__ Xn, const bf16* __restrict__ Wq,
                                                  const bf16* __restrict__ W1, const bf16* __restrict__ B1,
                                                  const bf16* __restrict__ Kp, const bf16* __restrict__ Vp,
                                                  bf16* __restrict__ A3) {
  using namespace k2;
  extern __shared__ __attribute__((aligned(16))) char sm[];
  const int tid = threadIdx.x, w = __builtin_amdgcn_readfirstlane(tid >> 6), lane = tid & 63, q = lane & 31, hh = lane >> 5;
  const int bid = blockIdx.x, xcd = bid & 7, jj = bid >> 3;
  const int h = xcd * 4 + (jj >> 3), ip = jj & 7;
  STAMP(dbg, 0);
  const bool heavy = w >= 4;
  const int r_own = heavy ? (63 - 4 * ip - (w - 4)) : (4 * ip + w);
  const int nT_own = (r_own >> 1) + 1;
  const int R0own = 32 * r_own;
  const unsigned sbase = lds_addr(sm);
  const int g16 = lane >> 4, qq = (lane >> 2) & 3, p4 = lane & 3;
  const unsigned vbase = sbase + p4 * 8 + qq * 64 + (g16 & 1) * 32 + hh * 256;
  const int kx0 = q * 256 + ((16 * hh) ^ ((q & 7) << 4));
  int koff[2], voff[2];
  for (int i = 0; i < 2; ++i) {
    const int pc = w + 8 * i;
    { const int row = 4 * pc + (lane >> 4), c = (lane & 15) ^ (row & 7);
      koff[i] = (row >> 4) * 2048 + (c >> 1) * 256 + (row & 15) * 16 + (c & 1) * 8; }
    { const int st = 2 * pc + (lane >> 5), key = (st >> 2) * 8 + ((lane >> 2) & 7), c = (st & 3) * 4 + (lane & 3);
      voff[i] = (key >> 4) * 2048 + (c >> 1) * 256 + (key & 15) * 16 + (c & 1) * 8; }
  }
  const bf16* __restrict__ Kh = Kp + (size_t)h * SEQ * HD;
  const bf16* __restrict__ Vh = Vp + (size_t)h * SEQ * HD;
#define DMA_K(tile, off_) do { dma16(Kh + (size_t)(tile) * 8192 + koff[0], sm + (off_) + w * 1024);        \
                               dma16(Kh + (size_t)(tile) * 8192 + koff[1], sm + (off_) + w * 1024 + 8192); } while (0)
#define DMA_V(tile, off_) do { dma16(Vh + (size_t)(tile) * 8192 + voff[0], sm + (off_) + w * 1024);        \
                               dma16(Vh + (size_t)(tile) * 8192 + voff[1], sm + (off_) + w * 1024 + 8192); } while (0)

  bf16x8 qf[8];
  {
    constexpr int PWQ = 0, PW1 = 65536;
    const unsigned wqbase = sbase + PWQ + hh * 2048 + qq * 64 + (g16 & 1) * 32 + p4 * 8;
    const unsigned w1base = sbase + PW1 + hh * 1024 + qq * 64 + (g16 & 1) * 32 + p4 * 8;
    f32x16 qa[4] = {}, ha[2] = {};
    const int own = own_f(h), own1 = own_f(h + 1);
    const bf16* xrow = Xn + (size_t)(R0own + q) * DM + hh * 8;
    const bf16* wq_src;
    {
      const int st = 2 * w + (lane >> 5), mr = (st >> 2) * 8 + ((lane >> 2) & 7), c8 = (st & 3) * 4 + (lane & 3);
      wq_src = Wq + (size_t)mr * 4096 + h * HD + c8 * 8;
    }
    const bf16* w1_src;
    {
      const int st = 2 * w + (lane >> 5), mr = (st >> 1) * 8 + ((lane >> 2) & 7), c8 = (st & 1) * 4 + (lane & 3);
      const int col = min(own + c8 * 8, FF - 8);
      w1_src = W1 + (size_t)mr * FF + col;
    }
#define DMA_W(c_)                                                                                       \
  do {                                                                                                  \
    const int cb_ = (c_) & 1;                                                                           \
    _Pragma("unroll") for (int i = 0; i < 4; ++i)                                                       \
      dma16(wq_src + (size_t)((c_) * 128 + 32 * i) * 4096, sm + PWQ + cb_ * 32768 + (w + 8 * i) * 1024); \
    _Pragma("unroll") for (int i = 0; i < 2; ++i)                                                       \
      dma16(w1_src + (size_t)((c_) * 128 + 64 * i) * FF, sm + PW1 + cb_ * 16384 + (w + 8 * i) * 1024);  \
  } while (0)
    bf16x8 xb[8], xbn[8];
    DMA_W(0);
    for (int ks = 0; ks < 8; ++ks) xb[ks] = *reinterpret_cast<const bf16x8*>(xrow + ks * 16);
    WAITV0();
    RAWBAR();
#pragma unroll 1
    for (int c = 0; c < 4; ++c) {
      const int cb = c & 1;
      if (c < 3) {
        DMA_W(c + 1);
        for (int ks = 0; ks < 8; ++ks) xbn[ks] = *reinterpret_cast<const bf16x8*>(xrow + (c + 1) * 128 + ks * 16);
      } else {
        DMA_K(0, OFF_KM);
      }
      SBAR();
      const unsigned wqp = wqbase + cb * 32768, w1p = w1base + cb * 16384;
      s16x4 fa[2][12];
#define RD_W(set, ks)                                                                                   \
  do {                                                                                                  \
    fa[set][0] = tr_asm<(ks) * 4096>(wqp); fa[set][1] = tr_asm<(ks) * 4096 + 256>(wqp);                 \
    fa[set][2] = tr_asm<(ks) * 4096 + 512>(wqp); fa[set][3] = tr_asm<(ks) * 4096 + 768>(wqp);           \
    fa[set][4] = tr_asm<(ks) * 4096 + 1024>(wqp); fa[set][5] = tr_asm<(ks) * 4096 + 1280>(wqp);         \
    fa[set][6] = tr_asm<(ks) * 4096 + 1536>(wqp); fa[set][7] = tr_asm<(ks) * 4096 + 1792>(wqp);         \
    fa[set][8] = tr_asm<(ks) * 2048>(w1p); fa[set][9] = tr_asm<(ks) * 2048 + 256>(w1p);                 \
    fa[set][10] = tr_asm<(ks) * 2048 + 512>(w1p); fa[set][11] = tr_asm<(ks) * 2048 + 768>(w1p);         \
  } while (0)
#define MM_W(set, ks)                                                                                   \
  do {                                                                                                  \
    _Pragma("unroll") for (int t = 0; t < 4; ++t)                                                       \
      qa[t] = __builtin_amdgcn_mfma_f32_32x32x16_bf16(cat8(fa[set][2 * t], fa[set][2 * t + 1]), xb[ks], qa[t], 0, 0, 0); \
    _Pragma("unroll") for (int u = 0; u < 2; ++u)                                                       \
      ha[u] = __builtin_amdgcn_mfma_f32_32x32x16_bf16(cat8(fa[set][8 + 2 * u], fa[set][9 + 2 * u]), xb[ks], ha[u], 0, 0, 0); \
  } while (0)
      RD_W(0, 0);
      RD_W(1, 1); WAITL(12); SBAR(); MM_W(0, 0); SBAR();
      RD_W(0, 2); WAITL(12); SBAR(); MM_W(1, 1); SBAR();
      RD_W(1, 3); WAITL(12); SBAR(); MM_W(0, 2); SBAR();
      RD_W(0, 4); WAITL(12); SBAR(); MM_W(1, 3); SBAR();
      RD_W(1, 5); WAITL(12); SBAR(); MM_W(0, 4); SBAR();
      RD_W(0, 6); WAITL(12); SBAR(); MM_W(1, 5); SBAR();
      RD_W(1, 7); WAITL(12); SBAR(); MM_W(0, 6); SBAR();
      WAITL(0); SBAR(); MM_W(1, 7); SBAR();
      if (c < 3) {
        WAITV0();
        RAWBAR();
        for (int ks = 0; ks < 8; ++ks) xb[ks] = xbn[ks];
      }
    }
#pragma unroll
    for (int t = 0; t < 4; ++t)
#pragma unroll
      for (int s2 = 0; s2 < 2; ++s2) {
        u32x4 v;
#pragma unroll
        for (int j = 0; j < 4; ++j) v[j] = pk2(qa[t][8 * s2 + 2 * j] * CEXP, qa[t][8 * s2 + 2 * j + 1] * CEXP);
        qf[2 * t + s2] = __builtin_bit_cast(bf16x8, v);
      }
    if (heavy) {
#pragma unroll
      for (int ds = 0; ds < 8; ++ds) *reinterpret_cast<bf16x8*>(sm + OFF_QS + (w - 4) * 8192 + ds * 1024 + lane * 16) = qf[ds];
    }
    bf16* hrow = A3 + (size_t)(R0own + q) * A3_LD + 4096;
#pragma unroll
    for (int u = 0; u < 2; ++u)
#pragma unroll
      for (int a4 = 0; a4 < 4; ++a4) {
        const int f0 = own + 32 * u + 8 * a4 + 4 * hh;
        if (f0 < own1) {
          const s16x4 bv = *reinterpret_cast<const s16x4*>(B1 + f0);
          float g[4];
#pragma unroll
          for (int b = 0; b < 4; ++b) g[b] = gelu_tanh(bfr(bfr(ha[u][4 * a4 + b]) + bf2f((unsigned short)bv[b])));
          u32x2 v = {pk2(g[0], g[1]), pk2(g[2], g[3])};
          *reinterpret_cast<u32x2*>(hrow + f0) = v;
        }
      }
    WAITV0();
    RAWBAR();
  }
  STAMP(dbg, 1);

  f32x16 o[4] = {};
  f32x16 s0, s1;
  f32x16 p0, p1;
  float m = 0.f, l = 0.f, alpha = 1.f;
  bool resc = false;
  bool first = true;
  const int a_sw = heavy ? 1000 : nT_own;
  int R0q = R0own;
  u32x4 pw[4];
  typedef float f32x2 __attribute__((ext_vector_type(2)));
  constexpr float THR = 8.f;

#define KFRAG(dst, kb_, ds)                                                                         \
  do {                                                                                              \
    const char* kp_ = (kb_) + (kx0 ^ (32 * ((ds) & 3))) + ((ds) >> 2) * 128;                        \
    dst[0] = *reinterpret_cast<const bf16x8*>(kp_);                                                 \
    dst[1] = *reinterpret_cast<const bf16x8*>(kp_ + 32 * 256);                                      \
  } while (0)
#define FIN_CHUNK(j)                                                                                \
  do {                                                                                              \
    p1[2 * (j)] = EXPF(p1[2 * (j)]);                                                                \
    p1[2 * (j) + 1] = EXPF(p1[2 * (j) + 1]);                                                        \
    psa += f32x2{p0[2 * (j)], p0[2 * (j) + 1]};                                                     \
    psb += f32x2{p1[2 * (j)], p1[2 * (j) + 1]};                                                     \
    pw[(j) >> 2][(j) & 3] = pk2(p0[2 * (j)], p0[2 * (j) + 1]);                                      \
    pw[2 + ((j) >> 2)][(j) & 3] = pk2(p1[2 * (j)], p1[2 * (j) + 1]);                                \
  } while (0)
#define RESCALE_O()                                                                                 \
  do {                                                                                              \
    if (resc) {                                                                                     \
      _Pragma("unroll") for (int t = 0; t < 4; ++t)                                                 \
        _Pragma("unroll") for (int r = 0; r < 16; ++r) o[t][r] *= alpha;                            \
      l *= alpha;                                                                                   \
    }                                                                                               \
  } while (0)
#define PHASE1_FULL(kOff_)                                                                          \
  do {                                                                                              \
    const char* kb_ = sm + (kOff_);                                                                 \
    bf16x8 kf[3][2];                                                                                \
    f32x2 psa = {0.f, 0.f}, psb = {0.f, 0.f};                                                       \
    KFRAG(kf[0], kb_, 0); KFRAG(kf[1], kb_, 1);                                                                           \
    RESCALE_O();                                                                                    \
    SBAR();                                                                                         \
    _Pragma("unroll") for (int ds = 0; ds < 8; ++ds) {                                              \
      if (ds + 2 < 8) KFRAG(kf[(ds + 2) % 3], kb_, ds + 2);                                         \
      if (ds == 0) {                                                                                \
        s0 = __builtin_amdgcn_mfma_f32_32x32x16_bf16(kf[0][0], qf[0], f32x16{}, 0, 0, 0);           \
        s1 = __builtin_amdgcn_mfma_f32_32x32x16_bf16(kf[0][1], qf[0], f32x16{}, 0, 0, 0);           \
      } else {                                                                                      \
        s0 = __builtin_amdgcn_mfma_f32_32x32x16_bf16(kf[ds % 3][0], qf[ds], s0, 0, 0, 0);           \
        s1 = __builtin_amdgcn_mfma_f32_32x32x16_bf16(kf[ds % 3][1], qf[ds], s1, 0, 0, 0);           \
      }                                                                                             \
      FIN_CHUNK(ds);                                                                                \
      SBAR();                                                                                       \
    }                                                                                               \
    psa += psb;                                                                                     \
    l += psa[0] + psa[1];                                                                           \
  } while (0)
#define PHASE1_QK_ONLY(kOff_)                                                                       \
  do {                                                                                              \
    const char* kb_ = sm + (kOff_);                                                                 \
    _Pragma("unroll") for (int ds = 0; ds < 8; ++ds) {                                              \
      bf16x8 kf[2];                                                                                 \
      KFRAG(kf, kb_, ds);                                                                           \
      s0 = __builtin_amdgcn_mfma_f32_32x32x16_bf16(kf[0], qf[ds], ds == 0 ? f32x16{} : s0, 0, 0, 0); \
      s1 = __builtin_amdgcn_mfma_f32_32x32x16_bf16(kf[1], qf[ds], ds == 0 ? f32x16{} : s1, 0, 0, 0); \
    }                                                                                               \
  } while (0)
#define PHASE1_FIN_ONLY()                                                                           \
  do {                                                                                              \
    f32x2 psa = {0.f, 0.f}, psb = {0.f, 0.f};                                                       \
    RESCALE_O();                                                                                    \
    _Pragma("unroll") for (int j = 0; j < 8; ++j) FIN_CHUNK(j);                                     \
    psa += psb;                                                                                     \
    l += psa[0] + psa[1];                                                                           \
  } while (0)
#define SM_MAX(tau_)                                                                                \
  do {                                                                                              \
    if (64 * (tau_) + 63 > R0q) {                                                                   \
      const int dq = R0q + q - 4 * hh - 64 * (tau_);                                                \
      _Pragma("unroll") for (int r = 0; r < 16; ++r) {                                              \
        const int c = (r & 3) + 8 * (r >> 2);                                                       \
        if (c > dq) s0[r] = -__builtin_inff();                                                      \
        if (c + 32 > dq) s1[r] = -__builtin_inff();                                                 \
      }                                                                                             \
    }                                                                                               \
    float tmax = s0[0];                                                                             \
    _Pragma("unroll") for (int r = 1; r < 16; ++r) tmax = fmaxf(tmax, s0[r]);                       \
    _Pragma("unroll") for (int r = 0; r < 16; ++r) tmax = fmaxf(tmax, s1[r]);                       \
    {                                                                                               \
      auto rr = __builtin_amdgcn_permlane32_swap(__float_as_uint(tmax), __float_as_uint(tmax), false, false); \
      tmax = fmaxf(__uint_as_float(rr[0]), __uint_as_float(rr[1]));                                 \
    }                                                                                               \
    resc = first || __any(tmax - m > THR);                                                          \
    if (resc) {                                                                                     \
      const float mnew = first ? tmax : fmaxf(tmax, m);                                             \
      alpha = first ? 0.f : __builtin_amdgcn_exp2f(m - mnew);                                       \
      m = mnew;                                                                                     \
      first = false;                                                                                \
    }                                                                                               \
  } while (0)
#define SM_EXP(lo, hi)                                                                              \
  do {                                                                                              \
    const f32x2 nm2 = {-m, -m};                                                                     \
    _Pragma("unroll") for (int r = (lo); r < (hi); r += 2) {                                        \
      const f32x2 t1 = f32x2{s1[r], s1[r + 1]} + nm2;                                               \
      const f32x2 t0 = f32x2{s0[r], s0[r + 1]} + nm2;                                               \
      p1[r] = t1[0]; p1[r + 1] = t1[1];                                                             \
      p0[r] = EXPF(t0[0]); p0[r + 1] = EXPF(t0[1]);                                                 \
    }                                                                                               \
  } while (0)
#define RD_V(set, ks, va_)                                                                          \
  do {                                                                                              \
    vf[set][0] = tr_asm<(8 * (ks) + 0) * 512>(va_); vf[set][1] = tr_asm<(8 * (ks) + 4) * 512>(va_); \
    vf[set][2] = tr_asm<(8 * (ks) + 1) * 512>(va_); vf[set][3] = tr_asm<(8 * (ks) + 5) * 512>(va_); \
    vf[set][4] = tr_asm<(8 * (ks) + 2) * 512>(va_); vf[set][5] = tr_asm<(8 * (ks) + 6) * 512>(va_); \
    vf[set][6] = tr_asm<(8 * (ks) + 3) * 512>(va_); vf[set][7] = tr_asm<(8 * (ks) + 7) * 512>(va_); \
  } while (0)
#define MM_V(set, ks)                                                                               \
  do {                                                                                              \
    _Pragma("unroll") for (int t = 0; t < 4; ++t)                                                   \
      o[t] = __builtin_amdgcn_mfma_f32_32x32x16_bf16(cat8(vf[set][2 * t], vf[set][2 * t + 1]),      \
                                                     __builtin_bit_cast(bf16x8, pw[ks]), o[t], 0, 0, 0); \
  } while (0)
#define PHASE2_FULL(vOff_, tau_)                                                                    \
  do {                                                                                              \
    const unsigned va = vbase + (vOff_);                                                            \
    s16x4 vf[2][8];                                                                                 \
    RD_V(0, 0, va);                                                                                 \
    RD_V(1, 1, va); WAITL(8); SBAR(); MM_V(0, 0); SM_MAX(tau_); SBAR();                             \
    RD_V(0, 2, va); WAITL(8); SBAR(); MM_V(1, 1); SM_EXP(0, 6); SBAR();                             \
    RD_V(1, 3, va); WAITL(8); SBAR(); MM_V(0, 2); SM_EXP(6, 12); SBAR();                            \
    WAITL(0); SBAR(); MM_V(1, 3); SM_EXP(12, 16); SBAR();                                           \
  } while (0)
#define PHASE2_PV_ONLY(vOff_)                                                                       \
  do {                                                                                              \
    const unsigned va = vbase + (vOff_);                                                            \
    s16x4 vf[2][8];                                                                                 \
    RD_V(0, 0, va);                                                                                 \
    RD_V(1, 1, va); WAITL(8); SBAR(); MM_V(0, 0); SBAR();                                           \
    RD_V(0, 2, va); WAITL(8); SBAR(); MM_V(1, 1); SBAR();                                           \
    RD_V(1, 3, va); WAITL(8); SBAR(); MM_V(0, 2); SBAR();                                           \
    WAITL(0); SBAR(); MM_V(1, 3); SBAR();                                                           \
  } while (0)
#define PHASE2_SM_ONLY(tau_)                                                                        \
  do { SM_MAX(tau_); SM_EXP(0, 16); } while (0)
#define STORE_ROWS(R0_)                                                                             \
  do {                                                                                              \
    float lt = l;                                                                                   \
    {                                                                                               \
      auto rr = __builtin_amdgcn_permlane32_swap(__float_as_uint(lt), __float_as_uint(lt), false, false); \
      lt = __uint_as_float(rr[0]) + __uint_as_float(rr[1]);                                         \
    }                                                                                               \
    const float inv = 1.f / lt;                                                                     \
    bf16* crow = A3 + (size_t)((R0_) + q) * A3_LD + h * HD + 4 * hh;                                \
    _Pragma("unroll") for (int t = 0; t < 4; ++t)                                                   \
      _Pragma("unroll") for (int a4 = 0; a4 < 4; ++a4) {                                            \
        float v4[4];                                                                                \
        _Pragma("unroll") for (int b = 0; b < 4; ++b) v4[b] = o[t][4 * a4 + b] * inv;               \
        u32x2 v = {pk2(v4[0], v4[1]), pk2(v4[2], v4[3])};                                           \
        *reinterpret_cast<u32x2*>(crow + 32 * t + 8 * a4) = v;                                      \
      }                                                                                             \
  } while (0)
#define HJOB(j) ((j) >= 2 * ip + 1 && (j) <= 15)
#define THJ(j) (((j) == 2 * ip + 1) ? (31 - 2 * ip) : ((j) + 15 - 2 * ip))

#define ISSUE_K(s_) do { if ((s_) + 1 <= 16) DMA_K((s_) + 1, OFF_KM + (((s_) + 1) & 1) * TB);                \
                         if (HJOB((s_) + 1)) DMA_K(THJ((s_) + 1), OFF_KH + (((s_) + 1) & 1) * TB); } while (0)
#define ISSUE_V(s_) do { DMA_V((s_), OFF_VM + ((s_) & 1) * TB);                                               \
                         if (HJOB(s_)) DMA_V(THJ(s_), OFF_VH + ((s_) & 1) * TB); } while (0)
#define RELOAD_Q() do { _Pragma("unroll") for (int ds = 0; ds < 8; ++ds)                                       \
      qf[ds] = *reinterpret_cast<const bf16x8*>(sm + OFF_QS + w * 8192 + ds * 1024 + lane * 16); } while (0)
#define PHASE_BAR() do { WAITV0(); RAWBAR(); } while (0)
#define PHASE_BAR_N(n_) do { if ((n_) == 4) asm volatile("s_waitcnt vmcnt(4)" ::: "memory");                 \
                             else if ((n_) == 2) asm volatile("s_waitcnt vmcnt(2)" ::: "memory");            \
                             else WAITV0();                                                                   \
                             RAWBAR(); } while (0)
  ISSUE_K(0);
  PHASE1_QK_ONLY(OFF_KM);
  if (!heavy && a_sw == 1) RELOAD_Q();
  PHASE_BAR();
  ISSUE_V(0);
  PHASE2_SM_ONLY(0);
  PHASE_BAR();
  float mprev = m;
#pragma unroll 1
  for (int s = 1; s <= 16; ++s) {
    const bool own_s = heavy || s < a_sw;
    const bool sw = !heavy && s == a_sw;
    const int tau = own_s ? s : THJ(s);
    ISSUE_K(s);
    PHASE1_FULL((own_s ? OFF_KM : OFF_KH) + (s & 1) * TB);
    if (!heavy && s == a_sw - 1) RELOAD_Q();
    TACC(ta0, tp);
    PHASE_BAR_N((s + 1 <= 16 ? 2 : 0) + (HJOB(s + 1) ? 2 : 0));
    TACC(ta1, tp);
    ISSUE_V(s);
    if (sw) { first = true; R0q = 2016 - R0own; }
    mprev = m;
    PHASE2_FULL(((heavy || s - 1 < a_sw) ? OFF_VM : OFF_VH) + ((s - 1) & 1) * TB, tau);
    if (sw) {
      STORE_ROWS(R0own);
#pragma unroll
      for (int t = 0; t < 4; ++t) o[t] = f32x16{};
      l = 0.f;
    }
    TACC(ta2, tp);
    PHASE_BAR_N(sw ? 0 : 2 + (HJOB(s) ? 2 : 0));
    TACC(ta3, tp);
  }
  if (!heavy) {
    char* pp = sm + OFF_PART + w * 17408 + lane * 16;
#pragma unroll
    for (int t = 0; t < 4; ++t)
#pragma unroll
      for (int a4 = 0; a4 < 4; ++a4)
        *reinterpret_cast<f32x4*>(pp + (4 * t + a4) * 1024) = f32x4{o[t][4 * a4], o[t][4 * a4 + 1], o[t][4 * a4 + 2], o[t][4 * a4 + 3]};
    *reinterpret_cast<f32x4*>(pp + 16 * 1024) = f32x4{a_sw == 16 ? -1e30f : mprev, l, 0.f, 0.f};
    PHASE_BAR();
  } else {
    PHASE_BAR();
    PHASE1_FIN_ONLY();
    PHASE2_PV_ONLY(OFF_VM);
  }
  STAMP(dbg, 2);
  if (heavy) {
    const char* pp = sm + OFF_PART + (w - 4) * 17408 + lane * 16;
    const f32x4 ml = *reinterpret_cast<const f32x4*>(pp + 16 * 1024);
    const float M = fmaxf(m, ml[0]);
    const float a1 = __builtin_amdgcn_exp2f(m - M), a2 = __builtin_amdgcn_exp2f(ml[0] - M);
    l = l * a1 + ml[1] * a2;
#pragma unroll
    for (int t = 0; t < 4; ++t)
#pragma unroll
      for (int a4 = 0; a4 < 4; ++a4) {
        const f32x4 po = *reinterpret_cast<const f32x4*>(pp + (4 * t + a4) * 1024);
#pragma unroll
        for (int b = 0; b < 4; ++b) o[t][4 * a4 + b] = o[t][4 * a4 + b] * a1 + po[b] * a2;
      }
    STORE_ROWS(R0own);
  }
  asm volatile("s_waitcnt vmcnt(0)" ::: "memory");
  STAMP(dbg, 3);
}

namespace k3 {
constexpr int STG = 32768, NSTG = 4, LDS_BYTES = STG * NSTG;
}
__global__ __launch_bounds__(512, 2) void k3_out(const bf16* __restrict__ A3, const bf16* __restrict__ Wo,
                                                 const bf16* __restrict__ W2, float* __restrict__ P3) {
  using namespace k3;
  extern __shared__ __attribute__((aligned(16))) char sm[];
  const int tid = threadIdx.x, wid = __builtin_amdgcn_readfirstlane(tid >> 6), lane = tid & 63, fr = lane & 15, fq = lane >> 4;
  const int bid = blockIdx.x, xcd = bid & 7, jj = bid >> 3;
  const int sp = xcd >> 1, rt = (xcd & 1) * 8 + (jj >> 2), ct = jj & 3;
  STAMP(dbg, 0);
  const int t0 = (sp == 0) ? 0 : (sp == 1 ? 22 : (sp == 2 ? 43 : 64));
  const int nt = (sp == 0 || sp == 3) ? 22 : 21;
  const int kg = wid >> 2, wm = (wid >> 1) & 1, wn = wid & 1;
  const int brow = rt * 128, bcol = ct * 128;
  int ar[2], ac[2], wk[2], wnn[2];
  for (int i = 0; i < 2; ++i) {
    k1::stage_rc(tid * 16 + i * 8192, ar[i], ac[i]);
    k1::stage_kn(tid * 16 + i * 8192, wk[i], wnn[i]);
  }
  auto stage = [&](int t, int buf) {
    char* sa = sm + buf * STG;
    char* sb = sa + 16384;
    const int k0 = (t0 + t) * 64;
    for (int i = 0; i < 2; ++i)
      __builtin_amdgcn_global_load_lds((const unsigned*)(A3 + (size_t)(brow + ar[i]) * A3_LD + k0 + ac[i]),
                                       (__attribute__((address_space(3))) unsigned*)(sa + tid * 16 + i * 8192), 16, 0, 0);
    for (int i = 0; i < 2; ++i) {
      const int k = k0 + wk[i];
      const bf16* src = (k < 4096) ? (Wo + (size_t)k * DM) : (W2 + (size_t)min(k - 4096, FF - 1) * DM);
      __builtin_amdgcn_global_load_lds((const unsigned*)(src + bcol + wnn[i]),
                                       (__attribute__((address_space(3))) unsigned*)(sb + tid * 16 + i * 8192), 16, 0, 0);
    }
  };
  const unsigned wb0 = lds_addr(sm) + 16384 + (4 * kg + fq) * 2048 + ((lane >> 2) & 3) * 32 + (lane & 3) * 8 + (fq & 1) * 128 + wn * 4 * 256;
  int ab[4];
  for (int mf = 0; mf < 4; ++mf) ab[mf] = k1::lds_byte(wm * 64 + mf * 16 + fr, kg * 32 + fq * 8);
  f32x4 acc[4][4] = {};
  stage(0, 0); stage(1, 1); stage(2, 2);
  bf16x8 xf[4], xn_[4];
  s16x4 wl[4], wh[4], wln[4], whn[4];
#define K3_READ(XF, WL, WH, t_)                                                                       \
  do {                                                                                                \
    const char* sa = sm + ((t_) & 3) * STG;                                                           \
    const unsigned a0 = wb0 + ((t_) & 3) * STG, a1 = a0 ^ 128;                                        \
    WL[0] = tr_asm<0>(a0); WH[0] = tr_asm<0>(a1); WL[1] = tr_asm<256>(a0); WH[1] = tr_asm<256>(a1);   \
    WL[2] = tr_asm<512>(a0); WH[2] = tr_asm<512>(a1); WL[3] = tr_asm<768>(a0); WH[3] = tr_asm<768>(a1); \
    _Pragma("unroll") for (int mf = 0; mf < 4; ++mf) XF[mf] = *reinterpret_cast<const bf16x8*>(sa + ab[mf]); \
  } while (0)
  asm volatile("s_waitcnt vmcnt(8)" ::: "memory");
  __builtin_amdgcn_s_barrier();
  K3_READ(xf, wl, wh, 0);
  asm volatile("s_waitcnt lgkmcnt(0)" ::: "memory");
  __builtin_amdgcn_sched_barrier(0);
  for (int t = 0; t < nt; ++t) {
    const bool more = t + 1 < nt;
    if (more) {
      if (t + 2 < nt) asm volatile("s_waitcnt vmcnt(4)" ::: "memory");
      else asm volatile("s_waitcnt vmcnt(0)" ::: "memory");
      __builtin_amdgcn_s_barrier();
      if (t + 3 < nt) stage(t + 3, (t + 3) & 3);
      K3_READ(xn_, wln, whn, t + 1);
    }
    __builtin_amdgcn_sched_barrier(0);
#pragma unroll
    for (int nf = 0; nf < 4; ++nf)
#pragma unroll
      for (int mf = 0; mf < 4; ++mf) acc[nf][mf] = __builtin_amdgcn_mfma_f32_16x16x32_bf16(cat8(wl[nf], wh[nf]), xf[mf], acc[nf][mf], 0, 0, 0);
    asm volatile("s_waitcnt lgkmcnt(0)" ::: "memory");
    __builtin_amdgcn_sched_barrier(0);
    if (more) {
#pragma unroll
      for (int i = 0; i < 4; ++i) { xf[i] = xn_[i]; wl[i] = wln[i]; wh[i] = whn[i]; }
    }
  }
  STAMP(dbg, 1);
  __syncthreads();
  if (kg == 1) {
#pragma unroll
    for (int nf = 0; nf < 4; ++nf)
#pragma unroll
      for (int mf = 0; mf < 4; ++mf) *reinterpret_cast<f32x4*>(sm + (((wid - 4) * 16 + nf * 4 + mf) * 64 + lane) * 16) = acc[nf][mf];
  }
  __syncthreads();
  if (kg == 0) {
    float* dst = P3 + (size_t)sp * SEQ * DM;
#pragma unroll
    for (int nf = 0; nf < 4; ++nf)
#pragma unroll
      for (int mf = 0; mf < 4; ++mf) {
        const f32x4 o = *reinterpret_cast<const f32x4*>(sm + ((wid * 16 + nf * 4 + mf) * 64 + lane) * 16);
        const int row = brow + wm * 64 + mf * 16 + fr, col = bcol + wn * 64 + nf * 16 + fq * 4;
        *reinterpret_cast<f32x4*>(dst + (size_t)row * DM + col) = acc[nf][mf] + o;
      }
  }
  asm volatile("s_waitcnt vmcnt(0)" ::: "memory");
  STAMP(dbg, 2);
}

__global__ __launch_bounds__(256) void k4_reduce(const float* __restrict__ P3, const bf16* __restrict__ X,
                                                 const bf16* __restrict__ B2, bf16* __restrict__ out) {
  const size_t i0 = ((size_t)blockIdx.x * 256 + threadIdx.x) * 8;
  const int n0 = (int)(i0 & (DM - 1));
  constexpr size_t PS = (size_t)SEQ * DM;
  f32x4 a[2], b[2];
  for (int hlf = 0; hlf < 2; ++hlf) {
    a[hlf] = *reinterpret_cast<const f32x4*>(P3 + i0 + 4 * hlf) + *reinterpret_cast<const f32x4*>(P3 + PS + i0 + 4 * hlf) +
             *reinterpret_cast<const f32x4*>(P3 + 2 * PS + i0 + 4 * hlf);
    b[hlf] = *reinterpret_cast<const f32x4*>(P3 + 3 * PS + i0 + 4 * hlf);
  }
  const bf16x8 xv = *reinterpret_cast<const bf16x8*>(X + i0);
  const bf16x8 bv = *reinterpret_cast<const bf16x8*>(B2 + n0);
  float r[8];
  for (int j = 0; j < 8; ++j) {
    const float av = bfr(a[j >> 2][j & 3]);
    const float mv = bfr(bfr(b[j >> 2][j & 3]) + bf2f((unsigned short)bv[j]));
    r[j] = bfr(bf2f((unsigned short)xv[j]) + av) + mv;
  }
  u32x4 o4 = {pk2(r[0], r[1]), pk2(r[2], r[3]), pk2(r[4], r[5]), pk2(r[6], r[7])};
  *reinterpret_cast<u32x4*>(out + i0) = o4;
}

extern "C" void kernel_launch(void* const* d_in, const int* in_sizes, int n_in, void* d_out, int out_size, void* d_ws,
                              size_t ws_size, hipStream_t stream) {
  const bf16* X = (const bf16*)d_in[0];
  const bf16* Ckv = (const bf16*)d_in[1];
  const bf16* Wq = (const bf16*)d_in[2];
  const bf16* Wuk = (const bf16*)d_in[3];
  const bf16* Wuv = (const bf16*)d_in[4];
  const bf16* Wo = (const bf16*)d_in[5];
  const bf16* W1 = (const bf16*)d_in[7];
  const bf16* B1 = (const bf16*)d_in[8];
  const bf16* W2 = (const bf16*)d_in[9];
  const bf16* B2 = (const bf16*)d_in[10];
  char* ws = (char*)d_ws;
  bf16* Xn = (bf16*)(ws + WS_XN);
  bf16* Kp = (bf16*)(ws + WS_K);
  bf16* Vp = (bf16*)(ws + WS_V);
  bf16* A3 = (bf16*)(ws + WS_A3);
  float* P3 = (float*)(ws + WS_P3);
  (void)hipFuncSetAttribute((const void*)k1_kvproj, hipFuncAttributeMaxDynamicSharedMemorySize, k1::LDS_BYTES);
  (void)hipFuncSetAttribute((const void*)k2_attn, hipFuncAttributeMaxDynamicSharedMemorySize, k2::LDS_BYTES);
  (void)hipFuncSetAttribute((const void*)k3_out, hipFuncAttributeMaxDynamicSharedMemorySize, k3::LDS_BYTES);
  k1_kvproj<<<256, 512, k1::LDS_BYTES, stream>>>(Ckv, Wuk, Wuv, Kp, Vp, X, Xn, A3);
  k2_attn<<<256, 512, k2::LDS_BYTES, stream>>>(Xn, Wq, W1, B1, Kp, Vp, A3);
  k3_out<<<256, 512, k3::LDS_BYTES, stream>>>(A3, Wo, W2, P3);
  k4_reduce<<<SEQ * DM / (256 * 8), 256, 0, stream>>>(P3, X, B2, (bf16*)d_out);
}
```
